```python
import torch

def _softmax(x, axis=-1):
    return torch.softmax(x, dim=axis)
def _gelu(x):
    return torch.nn.functional.gelu(x, approximate="tanh")
def _silu(x):
    return torch.nn.functional.silu(x)
def _sigmoid(x):
    return torch.sigmoid(x)
def _layernorm(x, axis=-1, eps=1e-5):
    m = x.mean(dim=axis, keepdim=True)
    v = x.var(dim=axis, keepdim=True, unbiased=False)
    return (x - m) / torch.sqrt(v + eps)
def _scatter_add(tgt, idx, src, axis=0):
    return tgt.clone().index_add_(axis, idx, src)
def _segment_sum(src, seg, n):
    out = torch.zeros((n,) + src.shape[1:], dtype=src.dtype)
    return out.index_add_(0, seg, src)
def _pad(x, pw):
    flat = tuple(v for pair in reversed(pw) for v in pair)
    return torch.nn.functional.pad(x, flat)

kernel_name = 'sequential_preLN__sliding_block_cached__dense_activation_squared_relu__decode'

def setup_inputs():

    _w = (lambda a: (lambda t: t.to(torch.bfloat16) if t.is_floating_point() else t)(torch.as_tensor(a)))
    def _A_setup():
        B, Sq, D = 1, 1, 576
        Hq, Hkv, Sk, Dh = 32, 16, 65536, 128
        assert Hq % Hkv == 0, (Hq, Hkv)
        return {
            "x": _w((lambda *s: torch.randn(*s, dtype=torch.bfloat16) * 0.05)(B, Sq, D)),
            "k_cache": _w((lambda *s: torch.randn(*s, dtype=torch.bfloat16) * 3.2)(B, Hkv, Sk, Dh)),
            "v_cache": _w((lambda *s: torch.randn(*s, dtype=torch.bfloat16) * 0.4)(B, Hkv, Sk, Dh)),
            "wq": _w((lambda *s: torch.randn(*s, dtype=torch.bfloat16) * 3.2)(D, Hq * Dh) * (D ** -0.5)),
            "wo": _w((lambda *s: torch.randn(*s, dtype=torch.bfloat16) * 0.4)(Hq * Dh, D) * ((Hq * Dh) ** -0.5)),
        }
    _A_in = _A_setup()

    def _B_setup():
        B, S, D, F = 1, 1, 576, 4608
        return {
            "x": _w((lambda *s: torch.randn(*s, dtype=torch.bfloat16) * 0.05)(B, S, D)),
            "w1": _w((lambda *s: torch.randn(*s, dtype=torch.bfloat16) * 0.05)(D, F)),
            "b1": _w((lambda *s: torch.randn(*s, dtype=torch.bfloat16) * 0.8)(F)),
            "w2": _w((lambda *s: torch.randn(*s, dtype=torch.bfloat16) * 0.05)(F, D)),
            "b2": _w((lambda *s: torch.randn(*s, dtype=torch.bfloat16) * 3.2)(D)),
        }
    _B_in = _B_setup()

    return {
        "A_x": _A_in["x"],
        "A_k_cache": _A_in["k_cache"],
        "A_v_cache": _A_in["v_cache"],
        "A_wq": _A_in["wq"],
        "A_wo": _A_in["wo"],
        "B_x": _B_in["x"],
        "B_w1": _B_in["w1"],
        "B_b1": _B_in["b1"],
        "B_w2": _B_in["w2"],
        "B_b2": _B_in["b2"],
    }

def reference(A_x, A_k_cache, A_v_cache, A_wq, A_wo, B_x, B_w1, B_b1, B_w2, B_b2):
    def _A_ref(x, k_cache, v_cache, wq, wo):
        B, Sq, D = x.shape
        Hq, Dh = 32, 128
        Hkv, Sk = k_cache.shape[1], k_cache.shape[2]
        W = min(4096, max(256, Sk // 4))
        rep = Hq // Hkv
        q = (lambda x, *d: x.permute(*d))((x @ wq).reshape(B, Sq, Hq, Dh), 0, 2, 1, 3)
        k = (lambda x, n, axis: torch.repeat_interleave(x, n, dim=axis))(k_cache[:, :, Sk - W:, :], rep, axis=1)
        v = (lambda x, n, axis: torch.repeat_interleave(x, n, dim=axis))(v_cache[:, :, Sk - W:, :], rep, axis=1)
        scores = torch.einsum("bhqd,bhkd->bhqk", q, k) * (Dh ** -0.5)
        p = _softmax(scores, axis=-1)
        o = torch.einsum("bhqk,bhkd->bhqd", p, v)
        return (lambda x, *d: x.permute(*d))(o, 0, 2, 1, 3).reshape(B, Sq, Hq * Dh) @ wo

    def _B_ref(x, w1, b1, w2, b2):
        h = torch.einsum("bsd,df->bsf", x, w1) + b1
        zero = torch.zeros_like(h)
        r = torch.where(h > zero, h, zero)
        h = r * r
        out = torch.einsum("bsf,fd->bsd", h, w2) + b2
        return out

    x = A_x
    h = x + _A_ref(_layernorm(x, axis=-1), A_k_cache, A_v_cache, A_wq, A_wo)
    h2 = h + _B_ref(_layernorm(h, axis=-1), B_w1, B_b1, B_w2, B_b2)
    return h2
```

```cpp
#include <hip/hip_runtime.h>
#include <hip/hip_bf16.h>
typedef __hip_bfloat16 bf;
__device__ __forceinline__ float b2f(bf v){return __bfloat162float(v);}
__device__ __forceinline__ float rb(float v){return __bfloat162float(__float2bfloat16(v));}
#define D 576
#define F 4608
#define SK 65536
#define W 4096
#define NS 16
__device__ float block_sum(float v, float* sh){
  for(int o=32;o>0;o>>=1) v+=__shfl_xor(v,o);
  int w=threadIdx.x>>6; __syncthreads(); if((threadIdx.x&63)==0) sh[w]=v; __syncthreads();
  float s=0; for(int i=0;i<(int)(blockDim.x>>6);i++) s+=sh[i]; return s;
}
__device__ void ln(float* sx, float* sh){
  float s=0; for(int i=threadIdx.x;i<D;i+=256) s+=sx[i];
  float m=block_sum(s,sh)/D;
  float v=0; for(int i=threadIdx.x;i<D;i+=256){float d=sx[i]-m; v+=d*d;}
  v=block_sum(v,sh)/D; float r=rsqrtf(v+1e-5f);
  for(int i=threadIdx.x;i<D;i+=256) sx[i]=rb((sx[i]-m)*r);
  __syncthreads();
}
__global__ void kq(const bf* x,const bf* wq,float* q){
  __shared__ float sx[D]; __shared__ float sh[8]; __shared__ float red[4][64];
  for(int i=threadIdx.x;i<D;i+=256) sx[i]=b2f(x[i]);
  __syncthreads(); ln(sx,sh);
  int c=blockIdx.x*64+(threadIdx.x&63), g=threadIdx.x>>6; float a=0;
  for(int r=g*144;r<g*144+144;r++) a+=sx[r]*b2f(wq[(size_t)r*4096+c]);
  red[g][threadIdx.x&63]=a; __syncthreads();
  if(g==0) q[c]=rb(red[0][threadIdx.x]+red[1][threadIdx.x]+red[2][threadIdx.x]+red[3][threadIdx.x]);
}
__global__ void kattn(const float* q,const bf* K,const bf* V,float* part){
  __shared__ float sq[256]; __shared__ float sp[2][256]; __shared__ float sh[8];
  int kvh=blockIdx.x/NS, sp_=blockIdx.x%NS, t=threadIdx.x;
  sq[t]=q[kvh*256+t]; __syncthreads();
  size_t base=((size_t)kvh*SK+(SK-W)+sp_*256)*128;
  const bf* kr=K+base+(size_t)t*128; float s0=0,s1=0;
  for(int d=0;d<128;d++){float k=b2f(kr[d]); s0+=sq[d]*k; s1+=sq[128+d]*k;}
  s0*=0.08838834764831845f; s1*=0.08838834764831845f;
  float m[2], l[2]; float sv[2]={s0,s1};
  for(int h=0;h<2;h++){ float v=sv[h]; for(int o=32;o>0;o>>=1) v=fmaxf(v,__shfl_xor(v,o));
    __syncthreads(); if((t&63)==0) sh[t>>6]=v; __syncthreads(); m[h]=fmaxf(fmaxf(sh[0],sh[1]),fmaxf(sh[2],sh[3]));
    float p=__expf(sv[h]-m[h]); sp[h][t]=p; l[h]=block_sum(p,sh);}
  __syncthreads();
  int h=t>>7,d=t&127; float o=0;
  for(int k=0;k<256;k++) o+=sp[h][k]*b2f(V[base+(size_t)k*128+d]);
  float* pp=part+((size_t)(kvh*2+h)*NS+sp_)*130; pp[d]=o; if(d==0){pp[128]=m[h];pp[129]=l[h];}
}
__global__ void kwo(const float* part,const bf* wo,float* hp){
  __shared__ float so[128]; int h=blockIdx.x,t=threadIdx.x; const float* pp=part+(size_t)h*NS*130;
  if(t<128){float M=-1e30f; for(int s=0;s<NS;s++) M=fmaxf(M,pp[s*130+128]); float L=0,o=0;
    for(int s=0;s<NS;s++){float w=__expf(pp[s*130+128]-M); L+=w*pp[s*130+129]; o+=w*pp[s*130+t];}
    so[t]=rb(o/L);} __syncthreads();
  for(int c=t;c<D;c+=256){float a=0; for(int d=0;d<128;d++) a+=so[d]*b2f(wo[(size_t)(h*128+d)*D+c]); hp[h*D+c]=a;}
}
__global__ void kffn(const bf* x,const float* hp,const bf* w1,const bf* b1,const bf* w2,float* op,float* hout){
  __shared__ float sx[D]; __shared__ float sh[8]; __shared__ float red[4][64]; __shared__ float s1[64];
  for(int i=threadIdx.x;i<D;i+=256){float a=0; for(int h=0;h<32;h++) a+=hp[h*D+i]; float hv=rb(b2f(x[i])+rb(a)); sx[i]=hv; if(blockIdx.x==0) hout[i]=hv;}
  __syncthreads(); ln(sx,sh);
  int c=blockIdx.x*64+(threadIdx.x&63), g=threadIdx.x>>6; float a=0;
  for(int r=g*144;r<g*144+144;r++) a+=sx[r]*b2f(w1[(size_t)r*F+c]);
  red[g][threadIdx.x&63]=a; __syncthreads();
  if(g==0){float v=rb(rb(red[0][threadIdx.x]+red[1][threadIdx.x]+red[2][threadIdx.x]+red[3][threadIdx.x])+b2f(b1[c])); v=fmaxf(v,0.f); s1[threadIdx.x]=rb(v*v);} __syncthreads();
  for(int d=threadIdx.x;d<D;d+=256){float o=0; for(int f=0;f<64;f++) o+=s1[f]*b2f(w2[(size_t)(blockIdx.x*64+f)*D+d]); op[blockIdx.x*D+d]=o;}
}
__global__ void kfin(const float* op,const float* hout,const bf* b2,bf* out){
  int d=blockIdx.x*64+threadIdx.x; float o=0; for(int b=0;b<72;b++) o+=op[b*D+d];
  out[d]=__float2bfloat16(hout[d]+rb(rb(o)+b2f(b2[d])));
}
extern "C" void kernel_launch(void* const* d_in, const int* in_sizes, int n_in, void* d_out, int out_size, void* d_ws, size_t ws_size, hipStream_t stream){
  const bf* x=(const bf*)d_in[0]; float* ws=(float*)d_ws;
  float* q=ws; float* part=ws+4096; float* hp=part+32*NS*130; float* op=hp+32*D; float* hout=op+72*D;
  kq<<<64,256,0,stream>>>(x,(const bf*)d_in[3],q);
  kattn<<<256,256,0,stream>>>(q,(const bf*)d_in[1],(const bf*)d_in[2],part);
  kwo<<<32,256,0,stream>>>(part,(const bf*)d_in[4],hp);
  kffn<<<72,256,0,stream>>>(x,hp,(const bf*)d_in[6],(const bf*)d_in[7],(const bf*)d_in[8],op,hout);
  kfin<<<9,64,0,stream>>>(op,hout,(const bf*)d_in[9],(bf*)d_out);
}
```

```cpp
#include <hip/hip_runtime.h>
#include <hip/hip_bf16.h>
typedef __hip_bfloat16 bf;
typedef unsigned u32; typedef unsigned long long u64;
typedef __attribute__((ext_vector_type(4))) unsigned uv4;
#define D 576
#define FF 4608
#define SK 65536
#define WIN 4096
__device__ __forceinline__ float rb(float v){return __bfloat162float(__float2bfloat16(v));}
__device__ __forceinline__ float lo(u32 v){return __uint_as_float(v<<16);}
__device__ __forceinline__ float hi(u32 v){return __uint_as_float(v&0xffff0000u);}
__device__ __forceinline__ void unp(uv4 v,float* f){f[0]=lo(v.x);f[1]=hi(v.x);f[2]=lo(v.y);f[3]=hi(v.y);f[4]=lo(v.z);f[5]=hi(v.z);f[6]=lo(v.w);f[7]=hi(v.w);}
__device__ __forceinline__ float ldbf(const bf* p){return __uint_as_float(((u32)*(const unsigned short*)p)<<16);}
__device__ __forceinline__ uv4 ldg(const void* p){return __builtin_nontemporal_load((const uv4*)p);}
__device__ __forceinline__ float wsum(float v){for(int o=32;o>0;o>>=1) v+=__shfl_xor(v,o); return v;}
__device__ __forceinline__ void bar(){asm volatile("" ::: "memory"); __syncthreads(); asm volatile("" ::: "memory");}
#define OFF_Q 4096
#define OFF_P (OFF_Q+64*4*32*16)
#define OFF_H (OFF_P+32*16*65*16)
#define OFF_O (OFF_H+8*288*16)
#define WS_END (OFF_O+72*288*16)
#define SPIN_CAP (1<<22)

__global__ void __launch_bounds__(576,3) mk(const bf* __restrict__ x,const bf* __restrict__ Kc,const bf* __restrict__ Vc,
    const bf* __restrict__ wq,const bf* __restrict__ wo,const bf* __restrict__ w1,const bf* __restrict__ b1,
    const bf* __restrict__ w2,const bf* __restrict__ b2,bf* __restrict__ out,char* ws){
  __shared__ float red[8][576]; __shared__ float sq[256]; __shared__ float sm[8][4]; __shared__ float sh[576]; __shared__ float s1[64];
  const int t=threadIdx.x, lane=t&63, w=t>>6, b=blockIdx.x;
  auto rs=__builtin_amdgcn_make_buffer_rsrc(ws,0,WS_END,0x00020000);
  auto gst=[&](int off,float a,float c,u64 tag){uv4 v; v.x=__float_as_uint(a); v.y=__float_as_uint(c); v.z=(u32)tag; v.w=(u32)(tag>>32); __builtin_amdgcn_raw_buffer_store_b128(v,rs,off,0,16);};
  auto gld=[&](int off){return __builtin_amdgcn_raw_buffer_load_b128(rs,off,0,16);};
  uv4 g0=gld(0); const u64 tag=(((u64)g0.y<<32)|g0.x)+1ull;
  auto okt=[&](uv4 v){return v.z==(u32)tag && v.w==(u32)(tag>>32);};
  const int kvh=b>>4, sp=b&15;
  const size_t kvbase=((size_t)kvh*SK+(SK-WIN)+sp*256+w*32)*128 + (size_t)(lane>>4)*128+(lane&15)*8;
  uv4 kr[8], vr[8];
  if(w<8){
    const int cg=b>>2, qt=b&3;
    float xs[9]; float s=0;
    #pragma unroll
    for(int i=0;i<9;i++){xs[i]=ldbf(x+i*64+lane); s+=xs[i];}
    uv4 wv[3]; float xr[3];
    #pragma unroll
    for(int i=0;i<3;i++){int j=w+8*i; if(j<18){int row=qt*144+j*8+(lane>>3); wv[i]=ldg(wq+(size_t)row*4096+cg*64+(lane&7)*8); xr[i]=ldbf(x+row);} else {wv[i]=uv4{0,0,0,0}; xr[i]=0;}}
    asm volatile("" ::: "memory");
    #pragma unroll
    for(int i=0;i<8;i++) kr[i]=ldg(Kc+kvbase+i*512);
    #pragma unroll
    for(int i=0;i<8;i++) vr[i]=ldg(Vc+kvbase+i*512);
    asm volatile("" ::: "memory");
    float m=wsum(s)*(1.f/D); float v=0;
    #pragma unroll
    for(int i=0;i<9;i++){float d=xs[i]-m; v+=d*d;}
    float r=rsqrtf(wsum(v)*(1.f/D)+1e-5f);
    float acc[8]={0,0,0,0,0,0,0,0};
    #pragma unroll
    for(int i=0;i<3;i++){float f[8]; unp(wv[i],f); float xn=rb((xr[i]-m)*r); if(w+8*i>=18) xn=0;
      #pragma unroll
      for(int c=0;c<8;c++) acc[c]+=xn*f[c];}
    #pragma unroll
    for(int c=0;c<8;c++){float a=acc[c]; a+=__shfl_xor(a,8); a+=__shfl_xor(a,16); a+=__shfl_xor(a,32); acc[c]=a;}
    if(lane<8){
      #pragma unroll
      for(int c=0;c<8;c++) red[w][lane*8+c]=acc[c];}
  }
  bar();
  if(w==0 && lane<32){float a=0,c=0;
    #pragma unroll
    for(int i=0;i<8;i++){a+=red[i][2*lane]; c+=red[i][2*lane+1];}
    gst(OFF_Q+(b*32+lane)*16,a,c,tag);}
  if(w==8){
    uv4 g[8]; int spins=0;
    while(true){ bool ok=true;
      #pragma unroll
      for(int j=0;j<8;j++){g[j]=gld(OFF_Q+(kvh*512+j*64+lane)*16); ok&=okt(g[j]);}
      if(__all(ok)||++spins>SPIN_CAP) break; asm volatile("" ::: "memory"); __builtin_amdgcn_s_sleep(1);}
    #pragma unroll
    for(int c=0;c<4;c++){float a=__uint_as_float(g[2*c].x)+__uint_as_float(g[2*c+1].x), e=__uint_as_float(g[2*c].y)+__uint_as_float(g[2*c+1].y);
      a+=__shfl_xor(a,32); e+=__shfl_xor(e,32);
      if(lane<32){sq[c*64+2*lane]=rb(a); sq[c*64+2*lane+1]=rb(e);}}
  }
  bar();
  const bool isF=b<72, isW=(b>=72&&b<144);
  const int hg=(b-72)/9, cs=(b-72)%9;
  uv4 ra[9], rc[9];
  if(w<8){
    float q0[8],q1[8];
    #pragma unroll
    for(int i=0;i<8;i++){q0[i]=sq[(lane&15)*8+i]; q1[i]=sq[128+(lane&15)*8+i];}
    float s0[8],s1v[8]; float m0=-1e30f,m1=-1e30f;
    #pragma unroll
    for(int i=0;i<8;i++){float f[8]; unp(kr[i],f); float a=0,c=0;
      #pragma unroll
      for(int d=0;d<8;d++){a+=q0[d]*f[d]; c+=q1[d]*f[d];}
      #pragma unroll
      for(int o=1;o<16;o<<=1){a+=__shfl_xor(a,o); c+=__shfl_xor(c,o);}
      a=rb(rb(a)*0.08838834764831845f); c=rb(rb(c)*0.08838834764831845f);
      s0[i]=a; s1v[i]=c; m0=fmaxf(m0,a); m1=fmaxf(m1,c);}
    asm volatile("" ::: "memory");
    if(isF){
      #pragma unroll
      for(int i=0;i<9;i++) ra[i]=ldg(w1+(size_t)(w*72+i*8+(lane>>3))*FF+b*64+(lane&7)*8);
      #pragma unroll
      for(int i=0;i<8;i++) rc[i]=ldg(w2+(size_t)(b*64+w*8+i)*D+lane*8);
      rc[8]=ldg(w2+(size_t)(b*64+w*8+(lane>>3))*D+512+(lane&7)*8);
    } else if(isW){
      #pragma unroll
      for(int i=0;i<8;i++) ra[i]=ldg(wo+(size_t)(hg*512+w*64+i*8+(lane>>3))*D+cs*64+(lane&7)*8);
    }
    asm volatile("" ::: "memory");
    m0=fmaxf(m0,__shfl_xor(m0,16)); m0=fmaxf(m0,__shfl_xor(m0,32));
    m1=fmaxf(m1,__shfl_xor(m1,16)); m1=fmaxf(m1,__shfl_xor(m1,32));
    float o0[8]={0,0,0,0,0,0,0,0},o1[8]={0,0,0,0,0,0,0,0}; float l0=0,l1=0;
    #pragma unroll
    for(int i=0;i<8;i++){float p0=__expf(s0[i]-m0),p1=__expf(s1v[i]-m1); l0+=p0; l1+=p1; float f[8]; unp(vr[i],f);
      #pragma unroll
      for(int d=0;d<8;d++){o0[d]+=p0*f[d]; o1[d]+=p1*f[d];}}
    l0+=__shfl_xor(l0,16); l0+=__shfl_xor(l0,32); l1+=__shfl_xor(l1,16); l1+=__shfl_xor(l1,32);
    #pragma unroll
    for(int d=0;d<8;d++){float a=o0[d]; a+=__shfl_xor(a,16); a+=__shfl_xor(a,32); float c=o1[d]; c+=__shfl_xor(c,16); c+=__shfl_xor(c,32);
      if(lane<16){red[w][lane*8+d]=a; red[w][128+lane*8+d]=c;}}
    if(lane==0){sm[w][0]=m0; sm[w][1]=m1; sm[w][2]=l0; sm[w][3]=l1;}
  }
  bar();
  if(t<130){
    int h=(t<128)?(t>>6):(t-128); float M=-1e30f;
    #pragma unroll
    for(int i=0;i<8;i++) M=fmaxf(M,sm[i][h]);
    float a=0,c=0,L=0; int pr=t&63;
    #pragma unroll
    for(int i=0;i<8;i++){float e=__expf(sm[i][h]-M); a+=e*red[i][h*128+2*pr]; c+=e*red[i][h*128+2*pr+1]; L+=e*sm[i][2+h];}
    int base=OFF_P+(((kvh*2+h)*16+sp)*65)*16;
    if(t<128) gst(base+pr*16,a,c,tag); else gst(base+64*16,M,L,tag);
  }
  if(!isF && !isW) return;
  bar();
  if(isW){
    if(w<8){
      int hd=hg*4+(w>>1); int base=OFF_P+((hd*16+(w&1)*8)*65)*16; uv4 g[8],gm; int spins=0;
      while(true){bool ok=true;
        #pragma unroll
        for(int j=0;j<8;j++){g[j]=gld(base+(j*65+lane)*16); ok&=okt(g[j]);}
        gm=gld(base+((lane&7)*65+64)*16); ok&=okt(gm);
        if(__all(ok)||++spins>SPIN_CAP) break; asm volatile("" ::: "memory"); __builtin_amdgcn_s_sleep(1);}
      float mm=__uint_as_float(gm.x), M=mm; M=fmaxf(M,__shfl_xor(M,1)); M=fmaxf(M,__shfl_xor(M,2)); M=fmaxf(M,__shfl_xor(M,4));
      float e=__expf(mm-M); float L=e*__uint_as_float(gm.y); L+=__shfl_xor(L,1); L+=__shfl_xor(L,2); L+=__shfl_xor(L,4);
      float a=0,c=0;
      #pragma unroll
      for(int j=0;j<8;j++){float ej=__shfl(e,j); a+=ej*__uint_as_float(g[j].x); c+=ej*__uint_as_float(g[j].y);}
      red[w][2*lane]=a; red[w][2*lane+1]=c; if(lane==0){sm[w][0]=M; sm[w][1]=L;}
    }
    bar();
    if(t<512){int hl=t>>7,d=t&127; float Ma=sm[2*hl][0],Mb=sm[2*hl+1][0],M=fmaxf(Ma,Mb); float ea=__expf(Ma-M),eb=__expf(Mb-M);
      float L=ea*sm[2*hl][1]+eb*sm[2*hl+1][1]; sh[t]=rb((ea*red[2*hl][d]+eb*red[2*hl+1][d])/L);}
    bar();
    float acc[8]={0,0,0,0,0,0,0,0};
    if(w<8){
      #pragma unroll
      for(int i=0;i<8;i++){float f[8]; unp(ra[i],f); float o=sh[w*64+i*8+(lane>>3)];
        #pragma unroll
        for(int c=0;c<8;c++) acc[c]+=o*f[c];}
      #pragma unroll
      for(int c=0;c<8;c++){float a=acc[c]; a+=__shfl_xor(a,8); a+=__shfl_xor(a,16); a+=__shfl_xor(a,32); acc[c]=a;}
    }
    bar();
    if(w<8&&lane<8){
      #pragma unroll
      for(int c=0;c<8;c++) red[w][lane*8+c]=acc[c];}
    bar();
    if(w==0&&lane<32){float a=0,c=0;
      #pragma unroll
      for(int i=0;i<8;i++){a+=red[i][2*lane]; c+=red[i][2*lane+1];}
      gst(OFF_H+(hg*288+cs*32+lane)*16,a,c,tag);}
    return;
  }
  if(w<8){
    uv4 g[5]; int spins=0;
    while(true){bool ok=true;
      #pragma unroll
      for(int j=0;j<5;j++){int gi=j*64+lane; if(gi<288){g[j]=gld(OFF_H+(w*288+gi)*16); ok&=okt(g[j]);}}
      if(__all(ok)||++spins>SPIN_CAP) break; asm volatile("" ::: "memory"); __builtin_amdgcn_s_sleep(1);}
    #pragma unroll
    for(int j=0;j<5;j++){int gi=j*64+lane; if(gi<288){red[w][2*gi]=__uint_as_float(g[j].x); red[w][2*gi+1]=__uint_as_float(g[j].y);}}
  }
  bar();
  { float a=0;
    #pragma unroll
    for(int i=0;i<8;i++) a+=red[i][t];
    sh[t]=rb(ldbf(x+t)+rb(a)); }
  bar();
  float acc[8]={0,0,0,0,0,0,0,0};
  if(w<8){
    float hs[9],s=0;
    #pragma unroll
    for(int i=0;i<9;i++){hs[i]=sh[i*64+lane]; s+=hs[i];}
    float m=wsum(s)*(1.f/D),v=0;
    #pragma unroll
    for(int i=0;i<9;i++){float d=hs[i]-m; v+=d*d;}
    float r=rsqrtf(wsum(v)*(1.f/D)+1e-5f);
    #pragma unroll
    for(int i=0;i<9;i++){float f[8]; unp(ra[i],f); float xn=rb((sh[w*72+i*8+(lane>>3)]-m)*r);
      #pragma unroll
      for(int c=0;c<8;c++) acc[c]+=xn*f[c];}
    #pragma unroll
    for(int c=0;c<8;c++){float a=acc[c]; a+=__shfl_xor(a,8); a+=__shfl_xor(a,16); a+=__shfl_xor(a,32); acc[c]=a;}
    if(lane<8){
      #pragma unroll
      for(int c=0;c<8;c++) red[w][lane*8+c]=acc[c];}
  }
  bar();
  if(t<64){float a=0;
    #pragma unroll
    for(int i=0;i<8;i++) a+=red[i][t];
    float v=rb(rb(a)+ldbf(b1+b*64+t)); v=fmaxf(v,0.f); s1[t]=rb(v*v);}
  bar();
  if(w<8){
    float a8[8]={0,0,0,0,0,0,0,0};
    #pragma unroll
    for(int i=0;i<8;i++){float f[8]; unp(rc[i],f); float hv=s1[w*8+i];
      #pragma unroll
      for(int c=0;c<8;c++) a8[c]+=hv*f[c];}
    float f[8]; unp(rc[8],f); float hv=s1[w*8+(lane>>3)];
    #pragma unroll
    for(int c=0;c<8;c++){red[w][lane*8+c]=a8[c]; float a=hv*f[c]; a+=__shfl_xor(a,8); a+=__shfl_xor(a,16); a+=__shfl_xor(a,32); if(lane<8) red[w][512+lane*8+c]=a;}
  }
  bar();
  if(t<288){float a=0,c=0;
    #pragma unroll
    for(int i=0;i<8;i++){a+=red[i][2*t]; c+=red[i][2*t+1];}
    gst(OFF_O+(b*288+t)*16,a,c,tag);}
  if(b>=9) return;
  bar();
  if(w<8){
    uv4 g[5]; int spins=0; float a=0,c=0;
    while(true){bool ok=true;
      #pragma unroll
      for(int j=0;j<5;j++){int fi=j*2+(lane>>5); if(fi<9){g[j]=gld(OFF_O+((w*9+fi)*288+b*32+(lane&31))*16); ok&=okt(g[j]);}}
      if(__all(ok)||++spins>SPIN_CAP) break; asm volatile("" ::: "memory"); __builtin_amdgcn_s_sleep(1);}
    #pragma unroll
    for(int j=0;j<5;j++){int fi=j*2+(lane>>5); if(fi<9){a+=__uint_as_float(g[j].x); c+=__uint_as_float(g[j].y);}}
    a+=__shfl_xor(a,32); c+=__shfl_xor(c,32);
    if(lane<32){red[w][2*lane]=a; red[w][2*lane+1]=c;}
  }
  bar();
  if(t<64){float a=0;
    #pragma unroll
    for(int i=0;i<8;i++) a+=red[i][t];
    int d=b*64+t; out[d]=__float2bfloat16(sh[d]+rb(rb(a)+ldbf(b2+d)));}
  if(b==0&&t==0){uv4 v; v.x=(u32)tag; v.y=(u32)(tag>>32); v.z=0; v.w=0; __builtin_amdgcn_raw_buffer_store_b128(v,rs,0,0,16);}
}
extern "C" void kernel_launch(void* const* d_in, const int* in_sizes, int n_in, void* d_out, int out_size, void* d_ws, size_t ws_size, hipStream_t stream){
  mk<<<256,576,0,stream>>>((const bf*)d_in[0],(const bf*)d_in[1],(const bf*)d_in[2],(const bf*)d_in[3],(const bf*)d_in[4],
    (const bf*)d_in[6],(const bf*)d_in[7],(const bf*)d_in[8],(const bf*)d_in[9],(bf*)d_out,(char*)d_ws);
}
```

```cpp
#include <hip/hip_runtime.h>
#include <hip/hip_bf16.h>
typedef __hip_bfloat16 bf;
typedef unsigned u32; typedef unsigned long long u64;
typedef __attribute__((ext_vector_type(4))) unsigned uv4;
#define D 576
#define FF 4608
#define SK 65536
#define WIN 4096
__device__ __forceinline__ float rb(float v){return __bfloat162float(__float2bfloat16(v));}
__device__ __forceinline__ float lo(u32 v){return __uint_as_float(v<<16);}
__device__ __forceinline__ float hi(u32 v){return __uint_as_float(v&0xffff0000u);}
__device__ __forceinline__ void unp(uv4 v,float* f){f[0]=lo(v.x);f[1]=hi(v.x);f[2]=lo(v.y);f[3]=hi(v.y);f[4]=lo(v.z);f[5]=hi(v.z);f[6]=lo(v.w);f[7]=hi(v.w);}
__device__ __forceinline__ float ldbf(const bf* p){return __uint_as_float(((u32)*(const unsigned short*)p)<<16);}
__device__ __forceinline__ uv4 ldg(const void* p){return __builtin_nontemporal_load((const uv4*)p);}
template<int C> __device__ __forceinline__ float dpp(float v){return __uint_as_float(__builtin_amdgcn_update_dpp(0,__float_as_uint(v),C,0xf,0xf,true));}
__device__ __forceinline__ float sum16(float a){a+=dpp<0xB1>(a); a+=dpp<0x4E>(a); a+=dpp<0x124>(a); a+=dpp<0x128>(a); return a;}
__device__ __forceinline__ float wsum(float v){v=sum16(v); v+=__shfl_xor(v,16); v+=__shfl_xor(v,32); return v;}
__device__ __forceinline__ void bar(){asm volatile("s_waitcnt lgkmcnt(0)\n s_barrier" ::: "memory");}
#define OFF_Q 4096
#define OFF_P (OFF_Q+64*4*32*16)
#define OFF_H (OFF_P+32*16*65*16)
#define OFF_O (OFF_H+8*288*16)
#define WS_END (OFF_O+72*288*16)
#define SPIN_CAP (1<<16)

__global__ void __launch_bounds__(576,3) mk(const bf* __restrict__ x,const bf* __restrict__ Kc,const bf* __restrict__ Vc,
    const bf* __restrict__ wq,const bf* __restrict__ wo,const bf* __restrict__ w1,const bf* __restrict__ b1,
    const bf* __restrict__ w2,const bf* __restrict__ b2,bf* __restrict__ out,char* ws){
  __shared__ float red[8][576]; __shared__ float sq[256]; __shared__ float sm[8][4]; __shared__ float sh[576]; __shared__ float s1[64];
  const int t=threadIdx.x, lane=t&63, w=t>>6, b=blockIdx.x;
  auto rs=__builtin_amdgcn_make_buffer_rsrc(ws,0,WS_END,0x00020000);
  auto gst=[&](int off,float a,float c,u64 tag){uv4 v; v.x=__float_as_uint(a); v.y=__float_as_uint(c); v.z=(u32)tag; v.w=(u32)(tag>>32); __builtin_amdgcn_raw_buffer_store_b128(v,rs,off,0,16);};
  auto gld=[&](int off){return __builtin_amdgcn_raw_buffer_load_b128(rs,off,0,16);};
  uv4 g0=gld(0); const u64 tag=(((u64)g0.y<<32)|g0.x)+1ull;
  auto okt=[&](uv4 v){return v.z==(u32)tag && v.w==(u32)(tag>>32);};
  const int kvh=b>>4, sp=b&15;
  const size_t kvbase=((size_t)kvh*SK+(SK-WIN)+sp*256+w*32)*128 + (size_t)(lane>>4)*128+(lane&15)*8;
  uv4 kr[8], vr[8];
  if(w<8){
    const int cg=b>>2, qt=b&3;
    float xs[9]; float s=0;
    #pragma unroll
    for(int i=0;i<9;i++){xs[i]=ldbf(x+i*64+lane); s+=xs[i];}
    uv4 wv[3]; float xr[3];
    #pragma unroll
    for(int i=0;i<3;i++){int j=w+8*i; if(j<18){int row=qt*144+j*8+(lane>>3); wv[i]=ldg(wq+(size_t)row*4096+cg*64+(lane&7)*8); xr[i]=ldbf(x+row);} else {wv[i]=uv4{0,0,0,0}; xr[i]=0;}}
    float m=wsum(s)*(1.f/D); float v=0;
    #pragma unroll
    for(int i=0;i<9;i++){float d=xs[i]-m; v+=d*d;}
    float r=rsqrtf(wsum(v)*(1.f/D)+1e-5f);
    float acc[8]={0,0,0,0,0,0,0,0};
    #pragma unroll
    for(int i=0;i<3;i++){float f[8]; unp(wv[i],f); float xn=rb((xr[i]-m)*r); if(w+8*i>=18) xn=0;
      #pragma unroll
      for(int c=0;c<8;c++) acc[c]+=xn*f[c];}
    #pragma unroll
    for(int c=0;c<8;c++){float a=acc[c]; a+=dpp<0x128>(a); a+=__shfl_xor(a,16); a+=__shfl_xor(a,32); acc[c]=a;}
    if(lane<8){
      #pragma unroll
      for(int c=0;c<8;c++) red[w][lane*8+c]=acc[c];}
  }
  bar();
  if(w==0 && lane<32){float a=0,c=0;
    #pragma unroll
    for(int i=0;i<8;i++){a+=red[i][2*lane]; c+=red[i][2*lane+1];}
    gst(OFF_Q+(b*32+lane)*16,a,c,tag);}
  if(w<8){
    asm volatile("" ::: "memory");
    #pragma unroll
    for(int i=0;i<8;i++) kr[i]=ldg(Kc+kvbase+i*512);
    asm volatile("" ::: "memory");
  }
  if(w==8){
    uv4 g[8]; int spins=0;
    while(true){ bool ok=true;
      #pragma unroll
      for(int j=0;j<8;j++){g[j]=gld(OFF_Q+(kvh*512+j*64+lane)*16); ok&=okt(g[j]);}
      if(__all(ok)||++spins>SPIN_CAP) break; asm volatile("" ::: "memory"); __builtin_amdgcn_s_sleep(1);}
    #pragma unroll
    for(int c=0;c<4;c++){float a=__uint_as_float(g[2*c].x)+__uint_as_float(g[2*c+1].x), e=__uint_as_float(g[2*c].y)+__uint_as_float(g[2*c+1].y);
      a+=__shfl_xor(a,32); e+=__shfl_xor(e,32);
      if(lane<32){sq[c*64+2*lane]=rb(a); sq[c*64+2*lane+1]=rb(e);}}
  }
  bar();
  const bool isF=b<72, isW=(b>=72&&b<144);
  const int hg=(b-72)/9, cs=(b-72)%9;
  uv4 ra[9], rc[9];
  if(w<8){
    #pragma unroll
    for(int i=0;i<8;i++) vr[i]=ldg(Vc+kvbase+i*512);
    asm volatile("" ::: "memory");
    float q0[8],q1[8];
    #pragma unroll
    for(int i=0;i<8;i++){q0[i]=sq[(lane&15)*8+i]; q1[i]=sq[128+(lane&15)*8+i];}
    float s0[8],s1v[8]; float m0=-1e30f,m1=-1e30f;
    #pragma unroll
    for(int i=0;i<8;i++){float f[8]; unp(kr[i],f); float a=0,c=0;
      #pragma unroll
      for(int d=0;d<8;d++){a+=q0[d]*f[d]; c+=q1[d]*f[d];}
      a=sum16(a); c=sum16(c);
      a=rb(rb(a)*0.08838834764831845f); c=rb(rb(c)*0.08838834764831845f);
      s0[i]=a; s1v[i]=c; m0=fmaxf(m0,a); m1=fmaxf(m1,c);}
    m0=fmaxf(m0,__shfl_xor(m0,16)); m0=fmaxf(m0,__shfl_xor(m0,32));
    m1=fmaxf(m1,__shfl_xor(m1,16)); m1=fmaxf(m1,__shfl_xor(m1,32));
    float o0[8]={0,0,0,0,0,0,0,0},o1[8]={0,0,0,0,0,0,0,0}; float l0=0,l1=0;
    #pragma unroll
    for(int i=0;i<8;i++){float p0=__expf(s0[i]-m0),p1=__expf(s1v[i]-m1); l0+=p0; l1+=p1; float f[8]; unp(vr[i],f);
      #pragma unroll
      for(int d=0;d<8;d++){o0[d]+=p0*f[d]; o1[d]+=p1*f[d];}}
    l0+=__shfl_xor(l0,16); l0+=__shfl_xor(l0,32); l1+=__shfl_xor(l1,16); l1+=__shfl_xor(l1,32);
    #pragma unroll
    for(int d=0;d<8;d++){float a=o0[d]; a+=__shfl_xor(a,16); a+=__shfl_xor(a,32); float c=o1[d]; c+=__shfl_xor(c,16); c+=__shfl_xor(c,32);
      if(lane<16){red[w][lane*8+d]=a; red[w][128+lane*8+d]=c;}}
    if(lane==0){sm[w][0]=m0; sm[w][1]=m1; sm[w][2]=l0; sm[w][3]=l1;}
    asm volatile("" ::: "memory");
    {
      const bf* pa=isF? w1+(size_t)(w*72+(lane>>3))*FF+b*64+(lane&7)*8 : (isW? wo+(size_t)(hg*512+w*64+(lane>>3))*D+cs*64+(lane&7)*8 : x);
      const size_t sa=isF? (size_t)8*FF : (isW? (size_t)8*D : 0);
      const bf* pc=isF? w2+(size_t)(b*64+w*8)*D+lane*8 : x; const size_t sc_=isF? D:0;
      const bf* pd=isF? w2+(size_t)(b*64+w*8+(lane>>3))*D+512+(lane&7)*8 : x;
      #pragma unroll
      for(int i=0;i<9;i++) ra[i]=ldg((i==8&&!isF)?x:pa+i*sa);
      #pragma unroll
      for(int i=0;i<8;i++) rc[i]=ldg(pc+i*sc_);
      rc[8]=ldg(pd);
    }
    asm volatile("" ::: "memory");
  }
  bar();
  if(t<130){
    int h=(t<128)?(t>>6):(t-128); float M=-1e30f;
    #pragma unroll
    for(int i=0;i<8;i++) M=fmaxf(M,sm[i][h]);
    float a=0,c=0,L=0; int pr=t&63;
    #pragma unroll
    for(int i=0;i<8;i++){float e=__expf(sm[i][h]-M); a+=e*red[i][h*128+2*pr]; c+=e*red[i][h*128+2*pr+1]; L+=e*sm[i][2+h];}
    int base=OFF_P+(((kvh*2+h)*16+sp)*65)*16;
    if(t<128) gst(base+pr*16,a,c,tag); else gst(base+64*16,M,L,tag);
  }
  if(!isF && !isW) return;
  bar();
  if(isW){
    if(w<8){
      int hd=hg*4+(w>>1); int base=OFF_P+((hd*16+(w&1)*8)*65)*16; uv4 g[8],gm; int spins=0;
      while(true){bool ok=true;
        #pragma unroll
        for(int j=0;j<8;j++){g[j]=gld(base+(j*65+lane)*16); ok&=okt(g[j]);}
        gm=gld(base+((lane&7)*65+64)*16); ok&=okt(gm);
        if(__all(ok)||++spins>SPIN_CAP) break; asm volatile("" ::: "memory"); __builtin_amdgcn_s_sleep(1);}
      float mm=__uint_as_float(gm.x), M=mm; M=fmaxf(M,__shfl_xor(M,1)); M=fmaxf(M,__shfl_xor(M,2)); M=fmaxf(M,__shfl_xor(M,4));
      float e=__expf(mm-M); float L=e*__uint_as_float(gm.y); L+=__shfl_xor(L,1); L+=__shfl_xor(L,2); L+=__shfl_xor(L,4);
      float a=0,c=0;
      #pragma unroll
      for(int j=0;j<8;j++){float ej=__shfl(e,j); a+=ej*__uint_as_float(g[j].x); c+=ej*__uint_as_float(g[j].y);}
      red[w][2*lane]=a; red[w][2*lane+1]=c; if(lane==0){sm[w][0]=M; sm[w][1]=L;}
    }
    bar();
    if(t<512){int hl=t>>7,d=t&127; float Ma=sm[2*hl][0],Mb=sm[2*hl+1][0],M=fmaxf(Ma,Mb); float ea=__expf(Ma-M),eb=__expf(Mb-M);
      float L=ea*sm[2*hl][1]+eb*sm[2*hl+1][1]; sh[t]=rb((ea*red[2*hl][d]+eb*red[2*hl+1][d])/L);}
    bar();
    float acc[8]={0,0,0,0,0,0,0,0};
    if(w<8){
      #pragma unroll
      for(int i=0;i<8;i++){float f[8]; unp(ra[i],f); float o=sh[w*64+i*8+(lane>>3)];
        #pragma unroll
        for(int c=0;c<8;c++) acc[c]+=o*f[c];}
      #pragma unroll
      for(int c=0;c<8;c++){float a=acc[c]; a+=dpp<0x128>(a); a+=__shfl_xor(a,16); a+=__shfl_xor(a,32); acc[c]=a;}
    }
    bar();
    if(w<8&&lane<8){
      #pragma unroll
      for(int c=0;c<8;c++) red[w][lane*8+c]=acc[c];}
    bar();
    if(w==0&&lane<32){float a=0,c=0;
      #pragma unroll
      for(int i=0;i<8;i++){a+=red[i][2*lane]; c+=red[i][2*lane+1];}
      gst(OFF_H+(hg*288+cs*32+lane)*16,a,c,tag);}
    return;
  }
  if(w<8){
    uv4 g[5]; int spins=0;
    while(true){bool ok=true;
      #pragma unroll
      for(int j=0;j<5;j++){int gi=j*64+lane; if(gi<288){g[j]=gld(OFF_H+(w*288+gi)*16); ok&=okt(g[j]);}}
      if(__all(ok)||++spins>SPIN_CAP) break; asm volatile("" ::: "memory"); __builtin_amdgcn_s_sleep(1);}
    #pragma unroll
    for(int j=0;j<5;j++){int gi=j*64+lane; if(gi<288){red[w][2*gi]=__uint_as_float(g[j].x); red[w][2*gi+1]=__uint_as_float(g[j].y);}}
  }
  bar();
  { float a=0;
    #pragma unroll
    for(int i=0;i<8;i++) a+=red[i][t];
    sh[t]=rb(ldbf(x+t)+rb(a)); }
  bar();
  float acc[8]={0,0,0,0,0,0,0,0};
  if(w<8){
    float hs[9],s=0;
    #pragma unroll
    for(int i=0;i<9;i++){hs[i]=sh[i*64+lane]; s+=hs[i];}
    float m=wsum(s)*(1.f/D),v=0;
    #pragma unroll
    for(int i=0;i<9;i++){float d=hs[i]-m; v+=d*d;}
    float r=rsqrtf(wsum(v)*(1.f/D)+1e-5f);
    #pragma unroll
    for(int i=0;i<9;i++){float f[8]; unp(ra[i],f); float xn=rb((sh[w*72+i*8+(lane>>3)]-m)*r);
      #pragma unroll
      for(int c=0;c<8;c++) acc[c]+=xn*f[c];}
    #pragma unroll
    for(int c=0;c<8;c++){float a=acc[c]; a+=dpp<0x128>(a); a+=__shfl_xor(a,16); a+=__shfl_xor(a,32); acc[c]=a;}
    if(lane<8){
      #pragma unroll
      for(int c=0;c<8;c++) red[w][lane*8+c]=acc[c];}
  }
  bar();
  if(t<64){float a=0;
    #pragma unroll
    for(int i=0;i<8;i++) a+=red[i][t];
    float v=rb(rb(a)+ldbf(b1+b*64+t)); v=fmaxf(v,0.f); s1[t]=rb(v*v);}
  bar();
  if(w<8){
    float a8[8]={0,0,0,0,0,0,0,0};
    #pragma unroll
    for(int i=0;i<8;i++){float f[8]; unp(rc[i],f); float hv=s1[w*8+i];
      #pragma unroll
      for(int c=0;c<8;c++) a8[c]+=hv*f[c];}
    float f[8]; unp(rc[8],f); float hv=s1[w*8+(lane>>3)];
    #pragma unroll
    for(int c=0;c<8;c++){red[w][lane*8+c]=a8[c]; float a=hv*f[c]; a+=dpp<0x128>(a); a+=__shfl_xor(a,16); a+=__shfl_xor(a,32); if(lane<8) red[w][512+lane*8+c]=a;}
  }
  bar();
  if(t<288){float a=0,c=0;
    #pragma unroll
    for(int i=0;i<8;i++){a+=red[i][2*t]; c+=red[i][2*t+1];}
    gst(OFF_O+(b*288+t)*16,a,c,tag);}
  if(b>=9) return;
  bar();
  if(w<8){
    uv4 g[5]; int spins=0; float a=0,c=0;
    while(true){bool ok=true;
      #pragma unroll
      for(int j=0;j<5;j++){int fi=j*2+(lane>>5); if(fi<9){g[j]=gld(OFF_O+((w*9+fi)*288+b*32+(lane&31))*16); ok&=okt(g[j]);}}
      if(__all(ok)||++spins>SPIN_CAP) break; asm volatile("" ::: "memory"); __builtin_amdgcn_s_sleep(1);}
    #pragma unroll
    for(int j=0;j<5;j++){int fi=j*2+(lane>>5); if(fi<9){a+=__uint_as_float(g[j].x); c+=__uint_as_float(g[j].y);}}
    a+=__shfl_xor(a,32); c+=__shfl_xor(c,32);
    if(lane<32){red[w][2*lane]=a; red[w][2*lane+1]=c;}
  }
  bar();
  if(t<64){float a=0;
    #pragma unroll
    for(int i=0;i<8;i++) a+=red[i][t];
    int d=b*64+t; out[d]=__float2bfloat16(sh[d]+rb(rb(a)+ldbf(b2+d)));}
  if(b==0&&t==0){uv4 v; v.x=(u32)tag; v.y=(u32)(tag>>32); v.z=0; v.w=0; __builtin_amdgcn_raw_buffer_store_b128(v,rs,0,0,16);}
}
extern "C" void kernel_launch(void* const* d_in, const int* in_sizes, int n_in, void* d_out, int out_size, void* d_ws, size_t ws_size, hipStream_t stream){
  mk<<<256,576,0,stream>>>((const bf*)d_in[0],(const bf*)d_in[1],(const bf*)d_in[2],(const bf*)d_in[3],(const bf*)d_in[4],
    (const bf*)d_in[6],(const bf*)d_in[7],(const bf*)d_in[8],(const bf*)d_in[9],(bf*)d_out,(char*)d_ws);
}
```
